```python
import torch

def _softmax(x, axis=-1):
    return torch.softmax(x, dim=axis)
def _gelu(x):
    return torch.nn.functional.gelu(x, approximate="tanh")
def _silu(x):
    return torch.nn.functional.silu(x)
def _sigmoid(x):
    return torch.sigmoid(x)
def _layernorm(x, axis=-1, eps=1e-5):
    m = x.mean(dim=axis, keepdim=True)
    v = x.var(dim=axis, keepdim=True, unbiased=False)
    return (x - m) / torch.sqrt(v + eps)
def _scatter_add(tgt, idx, src, axis=0):
    return tgt.clone().index_add_(axis, idx, src)
def _segment_sum(src, seg, n):
    out = torch.zeros((n,) + src.shape[1:], dtype=src.dtype)
    return out.index_add_(0, seg, src)
def _pad(x, pw):
    flat = tuple(v for pair in reversed(pw) for v in pair)
    return torch.nn.functional.pad(x, flat)

kernel_name = 'concat_heads__gqa_block_cached__mha_block_cached__near_ridge'

def setup_inputs():

    _w = (lambda a: (lambda t: t.to(torch.bfloat16) if t.is_floating_point() else t)(torch.as_tensor(a)))
    def _A_setup():
        B, Sq, D = 16, 128, 640
        Hq, Hkv, Sk, Dh = 32, 32, 512, 192
        assert Hq % Hkv == 0, (Hq, Hkv)
        return {
            "x": _w((lambda *s: torch.randn(*s, dtype=torch.bfloat16) * 0.8)(B, Sq, D)),
            "k_cache": _w((lambda *s: torch.randn(*s, dtype=torch.bfloat16) * 0.8)(B, Hkv, Sk, Dh)),
            "v_cache": _w((lambda *s: torch.randn(*s, dtype=torch.bfloat16) * 0.05)(B, Hkv, Sk, Dh)),
            "wq": _w((lambda *s: torch.randn(*s, dtype=torch.bfloat16) * 0.8)(D, Hq * Dh) * (D ** -0.5)),
            "wo": _w((lambda *s: torch.randn(*s, dtype=torch.bfloat16) * 0.05)(Hq * Dh, D) * ((Hq * Dh) ** -0.5)),
        }
    _A_in = _A_setup()

    def _B_setup():
        B, Sq, D = 16, 128, 640
        H, Sk, Dh = 32, 512, 192
        return {
            "x": _w((lambda *s: torch.randn(*s, dtype=torch.bfloat16) * 0.8)(B, Sq, D)),
            "k_cache": _w((lambda *s: torch.randn(*s, dtype=torch.bfloat16) * 0.8)(B, H, Sk, Dh)),
            "v_cache": _w((lambda *s: torch.randn(*s, dtype=torch.bfloat16) * 0.05)(B, H, Sk, Dh)),
            "wq": _w((lambda *s: torch.randn(*s, dtype=torch.bfloat16) * 0.8)(D, H * Dh) * (D ** -0.5)),
            "wo": _w((lambda *s: torch.randn(*s, dtype=torch.bfloat16) * 0.05)(H * Dh, D) * ((H * Dh) ** -0.5)),
        }
    _B_in = _B_setup()

    return {
        "A_x": _A_in["x"],
        "A_k_cache": _A_in["k_cache"],
        "A_v_cache": _A_in["v_cache"],
        "A_wq": _A_in["wq"],
        "A_wo": _A_in["wo"],
        "B_x": _B_in["x"],
        "B_k_cache": _B_in["k_cache"],
        "B_v_cache": _B_in["v_cache"],
        "B_wq": _B_in["wq"],
        "B_wo": _B_in["wo"],
    }

def reference(A_x, A_k_cache, A_v_cache, A_wq, A_wo, B_x, B_k_cache, B_v_cache, B_wq, B_wo):
    def _A_ref(x, k_cache, v_cache, wq, wo):
        B, Sq, D = x.shape
        Hq, Dh = 32, 192
        Hkv = k_cache.shape[1]
        rep = Hq // Hkv
        q = (lambda x, *d: x.permute(*d))((x @ wq).reshape(B, Sq, Hq, Dh), 0, 2, 1, 3)
        k = (lambda x, n, axis: torch.repeat_interleave(x, n, dim=axis))(k_cache, rep, axis=1)
        v = (lambda x, n, axis: torch.repeat_interleave(x, n, dim=axis))(v_cache, rep, axis=1)
        scores = torch.einsum("bhqd,bhkd->bhqk", q, k) * (Dh ** -0.5)
        p = _softmax(scores, axis=-1)
        o = torch.einsum("bhqk,bhkd->bhqd", p, v)
        return (lambda x, *d: x.permute(*d))(o, 0, 2, 1, 3).reshape(B, Sq, Hq * Dh) @ wo

    def _B_ref(x, k_cache, v_cache, wq, wo):
        B, Sq, D = x.shape
        H, Dh = 32, 192
        q = (lambda x, *d: x.permute(*d))((x @ wq).reshape(B, Sq, H, Dh), 0, 2, 1, 3)
        scores = torch.einsum("bhqd,bhkd->bhqk", q, k_cache) * (Dh ** -0.5)
        p = _softmax(scores, axis=-1)
        o = torch.einsum("bhqk,bhkd->bhqd", p, v_cache)
        return (lambda x, *d: x.permute(*d))(o, 0, 2, 1, 3).reshape(B, Sq, H * Dh) @ wo

    a = _A_ref(A_x, A_k_cache, A_v_cache, A_wq, A_wo)
    b = _B_ref(B_x, B_k_cache, B_v_cache, B_wq, B_wo)
    return torch.concatenate([a, b], axis=-1)
```

```cpp
#include <hip/hip_runtime.h>
#include <hip/hip_bf16.h>
#include <stdint.h>
#include <type_traits>
#define ROT 1
#define GOFF 0
#define YDELAY 0
#define WARM 0
#define WXNT 0

typedef __attribute__((ext_vector_type(8))) __bf16 bf16x8;
typedef __attribute__((ext_vector_type(4))) __bf16 bf16x4;
typedef __attribute__((ext_vector_type(2))) __bf16 bf16x2;
typedef __attribute__((ext_vector_type(16))) float f32x16;
typedef __attribute__((ext_vector_type(4))) float f32x4;
typedef __attribute__((ext_vector_type(2))) float f32x2;
typedef __attribute__((ext_vector_type(4))) unsigned u32x4;
#define LAS __attribute__((address_space(3)))
#define GAS __attribute__((address_space(1)))
#define DI __device__ __forceinline__
#define SB() __builtin_amdgcn_sched_barrier(0)
#define USEPRIO 0
#define PRIO(n) do { if (USEPRIO) __builtin_amdgcn_s_setprio(n); } while (0)
#define PIN(x) asm volatile("" : "+v"(x))
typedef __amdgpu_buffer_rsrc_t rsrc_t;
typedef LAS char* lptr;

constexpr int NB = 16, SQ = 128, DM = 640, NH = 32, SK = 512, DH = 192;
constexpr int HD = NH * DH;
constexpr int SLOT = 24576;
constexpr int WSUB = 12288;
constexpr int XOFF = 49152;
constexpr int K1_LDS = 163840;
constexpr unsigned EMPTY = 0xFFFFFFFFu;
constexpr float SM_C = 0.07216878364870322f * 1.4426950408889634f;

#define MFMA32(a, b, c) __builtin_amdgcn_mfma_f32_32x32x16_bf16((a), (b), (c), 0, 0, 0)

template <int I, int N, class F> DI void static_for(F&& f) { if constexpr (I < N) { f(std::integral_constant<int, I>{}); static_for<I + 1, N>(f); } }

DI rsrc_t mk_rsrc(const void* p, unsigned bytes) { return __builtin_amdgcn_make_buffer_rsrc((void*)p, 0, bytes, 0x00020000); }

template <int MOFF, int IMM, bool NT> DI void dma(unsigned mbase, unsigned voff, rsrc_t r, unsigned soff) {
  if constexpr (NT) asm volatile("s_add_u32 m0, %0, %5\n\ts_nop 0\n\tbuffer_load_dwordx4 %1, %2, %3 offen offset:%4 nt lds" :: "s"(mbase), "v"(voff), "s"(r), "s"(soff), "i"(IMM), "i"(MOFF) : "memory", "scc");
  else              asm volatile("s_add_u32 m0, %0, %5\n\ts_nop 0\n\tbuffer_load_dwordx4 %1, %2, %3 offen offset:%4 lds" :: "s"(mbase), "v"(voff), "s"(r), "s"(soff), "i"(IMM), "i"(MOFF) : "memory", "scc");
}
template <int IMM> DI void hidden_load16(bf16x8& o, unsigned voff, rsrc_t r, unsigned soff) {
  asm volatile("buffer_load_dwordx4 %0, %1, %2, %3 offen offset:%4" : "=&v"(o) : "v"(voff), "s"(r), "s"(soff), "i"(IMM) : "memory");
}
DI void load10_sc1(u32x4 (&d)[10], unsigned voff, rsrc_t r) {
  const unsigned v2 = voff + 4096, v3 = voff + 8192;
  asm volatile("buffer_load_dwordx4 %0, %10, %13, 0 offen sc1\n\tbuffer_load_dwordx4 %1, %10, %13, 0 offen offset:1024 sc1\n\t"
               "buffer_load_dwordx4 %2, %10, %13, 0 offen offset:2048 sc1\n\tbuffer_load_dwordx4 %3, %10, %13, 0 offen offset:3072 sc1\n\t"
               "buffer_load_dwordx4 %4, %11, %13, 0 offen sc1\n\tbuffer_load_dwordx4 %5, %11, %13, 0 offen offset:1024 sc1\n\t"
               "buffer_load_dwordx4 %6, %11, %13, 0 offen offset:2048 sc1\n\tbuffer_load_dwordx4 %7, %11, %13, 0 offen offset:3072 sc1\n\t"
               "buffer_load_dwordx4 %8, %12, %13, 0 offen sc1\n\tbuffer_load_dwordx4 %9, %12, %13, 0 offen offset:1024 sc1\n\ts_waitcnt vmcnt(0)"
               : "=&v"(d[0]), "=&v"(d[1]), "=&v"(d[2]), "=&v"(d[3]), "=&v"(d[4]), "=&v"(d[5]), "=&v"(d[6]), "=&v"(d[7]), "=&v"(d[8]), "=&v"(d[9])
               : "v"(voff), "v"(v2), "v"(v3), "s"(r) : "memory");
}
template <int N> DI void wait_bar() { asm volatile("s_waitcnt vmcnt(%0) lgkmcnt(0)\n\ts_barrier" :: "n"(N) : "memory"); }

DI bf16x8 lds128(lptr a) { return *(const LAS bf16x8*)a; }
template <int D2> DI bf16x8 ldstr(lptr a) {
  bf16x4 lo = __builtin_amdgcn_ds_read_tr16_b64_v4bf16((LAS bf16x4*)a);
  bf16x4 hi = __builtin_amdgcn_ds_read_tr16_b64_v4bf16((LAS bf16x4*)(a + D2));
  return __builtin_shufflevector(lo, hi, 0, 1, 2, 3, 4, 5, 6, 7);
}
DI unsigned cvtpk(float lo, float hi) { f32x2 v = {lo, hi}; bf16x2 b = __builtin_convertvector(v, bf16x2); return __builtin_bit_cast(unsigned, b); }
DI bf16x8 pack8(const f32x16& x, int s) {
  u32x4 w = {cvtpk(x[8 * s], x[8 * s + 1]), cvtpk(x[8 * s + 2], x[8 * s + 3]), cvtpk(x[8 * s + 4], x[8 * s + 5]), cvtpk(x[8 * s + 6], x[8 * s + 7])};
  return __builtin_bit_cast(bf16x8, w);
}
DI float swap_max(float v) { auto r = __builtin_amdgcn_permlane32_swap(__float_as_uint(v), __float_as_uint(v), false, false); return fmaxf(__uint_as_float(r[0]), __uint_as_float(r[1])); }
DI float swap_sum(float v) { auto r = __builtin_amdgcn_permlane32_swap(__float_as_uint(v), __float_as_uint(v), false, false); return __uint_as_float(r[0]) + __uint_as_float(r[1]); }

template <int P, bool NT> DI void tile_piece(unsigned lds_w  , const unsigned (&vo)[3], rsrc_t r, unsigned so0, unsigned so1) {
  constexpr int i = P % 3, hf = P / 3;
  dma<hf * 12288 + i * 1024, 0, NT>(lds_w, vo[i], r, hf ? so1 : so0);
}
template <bool KSWZ> DI unsigned piece_voff(int lane, int i, int par, unsigned stride) {
  const unsigned p = 1024 * i + 16 * lane, rowg = p / 384, cpos = (p - rowg * 384) >> 4;
  const unsigned swz = KSWZ ? 4 * par + (rowg >> 1) : ((rowg >> 1) & 1) << 2;
  return rowg * stride + 16 * (cpos ^ swz);
}

template <int N> DI bf16x8 trfrag(lptr e, lptr o) {
  constexpr int s = N / 6, dt = N % 6;
  return ldstr<1536>(((dt & 1) ? o : e) + (dt >> 1) * 128 + s * 6144);
}
template <int N> DI bf16x8 kfrag(lptr b0, lptr b1, lptr b2, lptr b3) {
  constexpr int kt = N / 12, s = N % 12;
  lptr b = (s & 3) == 0 ? b0 : (s & 3) == 1 ? b1 : (s & 3) == 2 ? b2 : b3;
  return lds128(b + (s >> 2) * 128 + kt * 12288);
}

__global__ __launch_bounds__(512, 2) void k1_qattn(const __bf16* __restrict__ xA, const __bf16* __restrict__ kA, const __bf16* __restrict__ vA, const __bf16* __restrict__ wqA,
                                                   const __bf16* __restrict__ xB, const __bf16* __restrict__ kB, const __bf16* __restrict__ vB, const __bf16* __restrict__ wqB,
                                                   __bf16* __restrict__ Ot, unsigned* __restrict__ part) {
  extern __shared__ __attribute__((aligned(16))) char smem[];
  const lptr L = (lptr)smem;
  const unsigned lds0 = (unsigned)(uintptr_t)L;
  const int tid = threadIdx.x, lane = tid & 63;
  const int w8 = __builtin_amdgcn_readfirstlane(tid >> 6);
  const int w = w8 & 3, un = w8 >> 2;
  const int bid = blockIdx.x, xcd = bid & 7, idx = bid >> 3;
  const int br = xcd >> 2, hd = (xcd & 3) * 8 + (idx & 7);
  {
    const u32x4 e4 = {EMPTY, EMPTY, EMPTY, EMPTY};
#pragma unroll
    for (int i = 0; i < 5; ++i) *(u32x4*)((char*)part + (size_t)bid * 40960 + i * 8192 + tid * 16) = e4;
  }
  const __bf16* x = br ? xB : xA; const __bf16* kc = br ? kB : kA; const __bf16* vc = br ? vB : vA; const __bf16* wq = br ? wqB : wqA;
  const rsrc_t rw = mk_rsrc(wq + hd * DH, (DM - 1) * HD * 2 + DH * 2);

  const unsigned vo_k[3] = {piece_voff<true>(lane, 0, w & 1, 384), piece_voff<true>(lane, 1, w & 1, 384), piece_voff<true>(lane, 2, w & 1, 384)};
  const unsigned vo_v[3] = {piece_voff<false>(lane, 0, 0, 384), piece_voff<false>(lane, 1, 0, 384), piece_voff<false>(lane, 2, 0, 384)};

  const int h = lane >> 5, r32 = lane & 31, g = (lane >> 4) & 1, q4 = (lane >> 2) & 3, p = lane & 3;
  const unsigned tr_e = (8 * h + q4) * 384 + 16 * (4 * (q4 >> 1) + 2 * g + (p & 1)) + 8 * (p >> 1);
  const int r23 = (r32 & 0x13) | ((r32 & 4) << 1) | ((r32 & 8) >> 1);
  const unsigned kb0 = r23 * 384 + 16 * (h ^ ((r23 >> 1) & 7));
#define IC(n) std::integral_constant<int, (n)>{}
  const unsigned sl0 = 114688 + un * SLOT, sl1 = un * XOFF, sl2 = SLOT + un * (81920 - SLOT);

#pragma unroll 1
  for (int rnd = 0; rnd < 2; ++rnd) {
  const int b = ((idx >> 3) + 4 * rnd) * 2 + un;
  int lq = lane; asm volatile("" : "+v"(lq));
  const unsigned vo_x0 = (lq >> 3) * 1280 + 16 * ((lq & 7) ^ (lq >> 4)), vo_x1 = vo_x0 ^ 64;
  const unsigned vo_w[3] = {piece_voff<false>(lq, 0, 0, HD * 2), piece_voff<false>(lq, 1, 0, HD * 2), piece_voff<false>(lq, 2, 0, HD * 2)};
  const int xr = 32 * w + (lq & 31);
  const unsigned xb0 = XOFF + un * 16384 + xr * 128 + 16 * ((lq >> 5) ^ ((xr >> 1) & 7));
  const rsrc_t rx = mk_rsrc(x + (size_t)b * SQ * DM, SQ * DM * 2);
  const rsrc_t rk = mk_rsrc(kc + (size_t)(b * NH + hd) * SK * DH, SK * DH * 2);
  const rsrc_t rv = mk_rsrc(vc + (size_t)(b * NH + hd) * SK * DH, SK * DH * 2);
  auto w_piece = [&](auto P, int t) { dma<P * 1024, 0, WXNT>(lds0 + (t & 1) * SLOT + w8 * 3072, vo_w[P], rw, (unsigned)(t * 64 + 8 * w8) * (HD * 2)); };
  auto x_piece = [&](auto P, int t) { dma<P * 1024, 0, WXNT>(lds0 + XOFF + (t & 1) * 32768 + un * 16384 + w * 4096, (P & 1) ? vo_x1 : vo_x0, rx, (unsigned)(32 * w) * 1280 + t * 128 + P * 8 * 1280); };
  const int rot = ROT ? (idx + 3 * un + (xcd << 1)) & 7 : 0;
  auto k0_piece = [&](int p) { const int hf = p >= 3, i = p - 3 * hf; dma<0, 0, true>(lds0 + sl0 + w * 3072 + hf * 12288 + i * 1024, i == 0 ? vo_k[0] : i == 1 ? vo_k[1] : vo_k[2], rk, (unsigned)(rot * 64 + 8 * w + 32 * hf) * 384); };
  auto k_piece = [&](auto P, int j, unsigned off) { const unsigned so = (unsigned)(((j + rot) & 7) * 64 + 8 * w) * 384; tile_piece<P, true>(lds0 + off + w * 3072, vo_k, rk, so, so + 32 * 384); };
  auto v_piece = [&](auto P, int j, unsigned off) { const unsigned so = (unsigned)(((j + rot) & 7) * 64 + 8 * w) * 384; tile_piece<P, true>(lds0 + off + w * 3072, vo_v, rv, so, so + 32 * 384); };

  f32x16 acc[6];
#pragma unroll
  for (int i = 0; i < 6; ++i) acc[i] = f32x16{};
  bf16x8 f[6];

  static_for<0, 3>([&](auto P) { w_piece(P, 0); });
  static_for<0, 4>([&](auto P) { x_piece(P, 0); });
  wait_bar<0>();
  bf16x8 xq0, xq1, xq2, xq3;
  {
    lptr we = L + tr_e, wo = L + (tr_e ^ 64);
    xq0 = lds128(L + xb0); xq1 = lds128(L + (xb0 ^ 32)); xq2 = lds128(L + (xb0 ^ 64)); xq3 = lds128(L + (xb0 ^ 96));
    static_for<0, 6>([&](auto I) { f[I] = trfrag<I>(we, wo); });
  }
  auto qstage = [&](int t, auto MODE) {
    constexpr int mode = MODE;
    const unsigned cs = (t & 1) * SLOT, ns = SLOT - cs;
    lptr we = L + (cs + tr_e), wo = L + (cs + (tr_e ^ 64));
    lptr nwe = L + (ns + tr_e), nwo = L + (ns + (tr_e ^ 64));
    lptr k0 = L + (sl0 + kb0), k1 = L + (sl0 + (kb0 ^ 32)), k2 = L + (sl0 + (kb0 ^ 64)), k3 = L + (sl0 + (kb0 ^ 96));
    const unsigned xnb = ((t + 1) & 1) * 32768 + xb0;
    bf16x8 xn0, xn1, xn2, xn3;
    static_for<0, 24>([&](auto I) {
      constexpr int n = I, s = n / 6, dt = n % 6;
      const bf16x8 xs = s == 0 ? xq0 : s == 1 ? xq1 : s == 2 ? xq2 : xq3;
      acc[dt] = MFMA32(f[n % 6], xs, acc[dt]); PIN(acc[dt]);
      if constexpr (n + 6 < 24) f[n % 6] = trfrag<(n + 6) % 24>(we, wo);
      if constexpr (mode == 3) {
        if constexpr (n < 3) w_piece(IC(n), 1);
        else if constexpr (n < 7) x_piece(IC(n - 3), 1);
        else if constexpr (n < 11) x_piece(IC(n - 7), 2);
      }
      if constexpr (n == 0 && mode != 2 && mode != 3) w_piece(IC(2), t + 1);
      if constexpr (n == 15 && mode == 0) k0_piece(t < 7 ? t - 1 : 5);
      if constexpr ((n == 3 || n == 6 || n == 9 || n == 12) && mode == 0) x_piece(IC(n / 3 - 1), t + 2);
      if constexpr ((n == 20 || n == 22) && (mode == 0 || mode == 3)) w_piece(IC((n - 20) / 2), t + 2);
      if constexpr (mode == 1 && (n == 20 || n == 22)) v_piece(IC((n - 20) / 2), 0, sl1);
      if constexpr (mode == 2 && n % 3 == 0 && n <= 9) v_piece(IC(2 + n / 3), 0, sl1);
      if constexpr (mode == 2 && n == 20) k_piece(IC(0), 1, sl2);
      if constexpr (n == 19) {
        if constexpr (mode == 0) wait_bar<5>(); else if constexpr (mode == 3) wait_bar<4>(); else if constexpr (mode == 1) wait_bar<0>(); else wait_bar<63>();
        if constexpr (mode != 2) { f[0] = trfrag<0>(nwe, nwo); f[1] = trfrag<1>(nwe, nwo); }
        else { f[0] = kfrag<0>(k0, k1, k2, k3); f[1] = kfrag<1>(k0, k1, k2, k3); }
      }
      if constexpr (n >= 20) { if constexpr (mode != 2) f[n % 6] = trfrag<n - 18>(nwe, nwo); else f[n % 6] = kfrag<n - 18>(k0, k1, k2, k3); }
      if constexpr (mode != 2) {
        if constexpr (n == 19) xn0 = lds128(L + xnb);
        if constexpr (n == 20) xn1 = lds128(L + (xnb ^ 32));
        if constexpr (n == 21) xn2 = lds128(L + (xnb ^ 64));
        if constexpr (n == 22) xn3 = lds128(L + (xnb ^ 96));
      }
      SB();
    });
    if constexpr (mode != 2) { xq0 = xn0; xq1 = xn1; xq2 = xn2; xq3 = xn3; }
  };
  qstage(0, IC(3));
#pragma unroll 1
  for (int t = 1; t < 8; ++t) qstage(t, IC(0));
  qstage(8, IC(1));
  qstage(9, IC(2));

  bf16x8 qf[12];
#pragma unroll
  for (int i = 0; i < 6; ++i) {
#pragma unroll
    for (int r = 0; r < 16; ++r) acc[i][r] *= SM_C;
    qf[2 * i] = pack8(acc[i], 0); qf[2 * i + 1] = pack8(acc[i], 1);
  }
#pragma unroll
  for (int i = 0; i < 6; ++i) acc[i] = f32x16{};

  constexpr float PMAX = 256.0f;
  float mref = 0.f, l_run = 0.f, dst = 0.f;
  f32x16 s0 = f32x16{};
  float rs0, rs1, alpha, pa, pb; int pend = 0;
  u32x4 pw[2];
  auto sm_slice = [&](auto G, auto SECOND, f32x16& s, int first) {
    constexpr int g = G; constexpr bool second = SECOND;
    if constexpr (g == 1) { rs0 = 0.f; rs1 = 0.f; }
    if constexpr (g >= 2 && g <= 9) {
      constexpr int e = 2 * (g - 2);
      if constexpr (g == 2) { rs0 = pa; rs1 = pb; } else { rs0 += pa; rs1 += pb; }
      pw[e >> 3][(e >> 1) & 3] = cvtpk(pa, pb);
      asm volatile("" : "+v"(pw[e >> 3]), "+v"(rs0), "+v"(rs1));
    }
    if constexpr (g >= 1 && g <= 8) {
      constexpr int e = 2 * (g - 1);
      pa = __builtin_amdgcn_exp2f(s[e]); pb = __builtin_amdgcn_exp2f(s[e + 1]);
      asm volatile("" : "+v"(pa), "+v"(pb));
    }
    if constexpr (g == 10) {
      float rs = rs0 + rs1;
      const int stale = second ? pend : 0;
      const int go = (__builtin_amdgcn_ballot_w64(!(rs <= PMAX)) != 0) | stale | first;
      alpha = 1.f; pend = 0;
      const float din = second ? dst : 0.f;
      dst = 0.f;
      if (__builtin_expect(go, 0)) {
        float mx = s[0];
#pragma unroll
        for (int r = 1; r < 16; ++r) mx = fmaxf(mx, s[r]);
        mx = swap_max(mx) - din;
        const float delta = first ? mx : fmaxf(mx, 0.f);
        const float adj = din + delta;
        rs = 0.f;
#pragma unroll
        for (int e = 0; e < 16; e += 2) {
          const float p0 = __builtin_amdgcn_exp2f(s[e] - adj), p1 = __builtin_amdgcn_exp2f(s[e + 1] - adj);
          rs += p0 + p1; pw[e >> 3][(e >> 1) & 3] = cvtpk(p0, p1);
        }
        mref += delta;
        alpha = first ? 0.f : __builtin_amdgcn_exp2f(-delta);
        dst = delta; pend = 1;
      }
      l_run = l_run * alpha + rs;
      asm volatile("" : "+v"(l_run));
    }
  };
  auto rescale = [&]() {
    if (__builtin_expect(pend, 0)) {
#pragma unroll
      for (int dt = 0; dt < 6; ++dt)
#pragma unroll
        for (int r = 0; r < 16; ++r) acc[dt][r] *= alpha;
    }
  };
  unsigned ok = sl0, ov = sl1, on = sl2;
  auto att_tile = [&](int j, auto REM) {
    constexpr int rem = REM;
    lptr k0 = L + (ok + kb0), k1 = L + (ok + (kb0 ^ 32)), k2 = L + (ok + (kb0 ^ 64)), k3 = L + (ok + (kb0 ^ 96));
    lptr ve = L + (ov + tr_e), vo = L + (ov + (tr_e ^ 64));
    f32x16 s1;
    bf16x8 pf[4];
    static_for<0, 24>([&](auto I) {
      constexpr int n = I;
      if constexpr (n == 12) PRIO(1);
      if constexpr (n < 12) { s0 = MFMA32(f[n % 6], qf[n], s0); PIN(s0); }
      else { s1 = MFMA32(f[n % 6], qf[n - 12], s1); PIN(s1); }
      if constexpr (n >= 2 && n < 10) { s1[2 * (n - 2)] = -mref; s1[2 * (n - 2) + 1] = -mref; if constexpr (n == 9) PIN(s1); }
      if constexpr (n + 6 < 24) f[n % 6] = kfrag<(n + 6) % 24>(k0, k1, k2, k3);
      if constexpr (n == 19) {
        if constexpr (rem >= 1) wait_bar<6>(); else wait_bar<0>();
        f[0] = trfrag<0>(ve, vo); f[1] = trfrag<1>(ve, vo);
      }
      if constexpr (n >= 20) f[n % 6] = trfrag<n - 18>(ve, vo);
      if constexpr (n % 4 == 0 && n <= 16 && rem >= 1) k_piece(IC(1 + n / 4), j + 1, on);
      if constexpr (n == 20 && rem >= 1) v_piece(IC(0), j + 1, ok);
      if constexpr (n >= 12) sm_slice(IC(n - 12), std::false_type{}, s0, j == 0);
      SB();
    });
    rescale();
    pf[0] = __builtin_bit_cast(bf16x8, pw[0]); pf[1] = __builtin_bit_cast(bf16x8, pw[1]);
    SB();
    lptr n0 = L + (on + kb0), n1 = L + (on + (kb0 ^ 32)), n2 = L + (on + (kb0 ^ 64)), n3 = L + (on + (kb0 ^ 96));
    static_for<0, 24>([&](auto I) {
      constexpr int n = I, s = n / 6, dt = n % 6;
      if constexpr (n == 12) { PRIO(0); rescale(); pf[2] = __builtin_bit_cast(bf16x8, pw[0]); pf[3] = __builtin_bit_cast(bf16x8, pw[1]); SB(); }
      acc[dt] = MFMA32(f[n % 6], pf[s], acc[dt]); PIN(acc[dt]);
      if constexpr (n + 6 < 24) f[n % 6] = trfrag<(n + 6) % 24>(ve, vo);
      if constexpr (n == 19) {
        if constexpr (rem >= 1) { wait_bar<6>(); f[0] = kfrag<0>(n0, n1, n2, n3); f[1] = kfrag<1>(n0, n1, n2, n3); }
        else if (GOFF) wait_bar<63>();
      }
      if constexpr (n >= 20 && rem >= 1) f[n % 6] = kfrag<n - 18>(n0, n1, n2, n3);
      if constexpr (n % 4 == 0 && n <= 16 && rem >= 1) v_piece(IC(1 + n / 4), j + 1, ok);
      if constexpr (n == 20 && rem >= 2) k_piece(IC(0), j + 2, ov);
      if constexpr (n < 12) sm_slice(IC(n), std::true_type{}, s1, 0);
      if constexpr (n >= 13 && n < 21 && rem >= 1) { s0[2 * (n - 13)] = -mref; s0[2 * (n - 13) + 1] = -mref; if constexpr (n == 20) PIN(s0); }
      SB();
    });
    { const unsigned t_ = ok; ok = on; on = ov; ov = t_; }
  };
#pragma unroll 1
  for (int j = 0; j < 6; ++j) att_tile(j, IC(2));
  att_tile(6, IC(1));
  att_tile(7, IC(0));
  const float inv = 1.0f / swap_sum(l_run);
  int le = lane; asm volatile("" : "+v"(le));
  char* ob = (char*)Ot + ((size_t)((br * NB + b) * 4 + w) * 384 + hd * 12) * 1024 + le * 16;
#pragma unroll
  for (int dt = 0; dt < 6; ++dt) {
#pragma unroll
    for (int r = 0; r < 16; ++r) acc[dt][r] *= inv;
    *(bf16x8*)(ob + (2 * dt) * 1024) = pack8(acc[dt], 0);
    *(bf16x8*)(ob + (2 * dt + 1) * 1024) = pack8(acc[dt], 1);
  }
  asm volatile("s_waitcnt lgkmcnt(0)\n\ts_barrier" ::: "memory");
  }
}

constexpr int K2_STAGE = 40960, K2_LDS = 3 * K2_STAGE;
#define K2TOUCH 0
#define K2D 5

__global__ __launch_bounds__(512, 2) void k2_oproj(const __bf16* __restrict__ Ot, const __bf16* __restrict__ woA, const __bf16* __restrict__ woB,
                                                   __bf16* __restrict__ out, float* __restrict__ part) {
  extern __shared__ __attribute__((aligned(16))) char smem[];
  const lptr L = (lptr)smem;
  const unsigned lds0 = (unsigned)(uintptr_t)L;
  const int tid = threadIdx.x, lane = tid & 63;
  const int w = __builtin_amdgcn_readfirstlane(tid >> 6);
  const int bid = blockIdx.x, xcd = bid & 7, idx = bid >> 3;
  const int br = xcd >> 2, kh = xcd & 1, b = ((xcd >> 1) & 1) * 8 + (idx >> 2), nt = idx & 3;
  const int bb = br * 16 + b;
  const int rbk = w & 3, ks2 = w >> 2;
  const __bf16* wo = br ? woB : woA;
  const rsrc_t rw = mk_rsrc(wo, HD * DM * 2);
  const int k0 = kh * 3072 + ks2 * 1536;
  unsigned vo_w[5];
#pragma unroll
  for (int i = 0; i < 5; ++i) { const unsigned p = 1024 * i + 16 * lane, r = p / 320; vo_w[i] = r * 1280 + (p - r * 320); }
  const unsigned so0 = (unsigned)(k0 + 16 * rbk) * 1280 + nt * 320;
  const unsigned m0b = lds0 + ks2 * 20480 + rbk * 5120;
  auto w_piece = [&](auto P, int st, int slot) { dma<P * 1024, 0, false>(m0b + slot * K2_STAGE, vo_w[P], rw, so0 + (unsigned)st * 64 * 1280); };
  const rsrc_t ro = mk_rsrc((const char*)Ot + ((size_t)(bb * 4 + rbk) * 384 + k0 / 16) * 1024, 96 * 1024);
  const unsigned vo_o = lane * 16;
  const int h = lane >> 5, g = (lane >> 4) & 1, q4 = (lane >> 2) & 3, p = lane & 3;
  const int sp = ((p & 1) << 1) | (p >> 1);
  const unsigned trb = ks2 * 20480 + (8 * h + q4) * 320 + 32 * g + 8 * sp;
  const char* tch0 = lane < 32 ? (const char*)Ot + ((size_t)(bb * 4 + rbk) * 384 + k0 / 16) * 1024 + nt * 4096 + lane * 128
                               : (const char*)wo + (size_t)(kh * 3072) * 1280 + ((size_t)((idx * 8 + w) * 20 + (lane < 52 ? lane - 32 : 19))) * 128;
  {
    const unsigned li = (idx * 8 + w) * 20 + (lane < 52 && lane >= 32 ? lane - 32 : 19);
    if (lane >= 32) tch0 = (const char*)wo + ((size_t)(kh * 3072 + (li / 2560) * 1536) * 1280) + (size_t)(li % 2560) * 128;
  }
  const unsigned tstep = lane < 32 ? 4096u : 64u * 1280u;
  unsigned tdummy = 0;
  auto touch = [&](int st) { const char* a_ = tch0 + (size_t)st * tstep; asm volatile("global_load_dword %0, %1, off" : "+v"(tdummy) : "v"(a_) : "memory"); };

  f32x16 acc[5];
#pragma unroll
  for (int i = 0; i < 5; ++i) acc[i] = f32x16{};
  bf16x8 of[3][4];
  bf16x8 f[5];
  auto o_load = [&](auto S, int st, bf16x8& o) { hidden_load16<S * 1024>(o, vo_o, ro, st * 4096); };
  auto wfrag = [&](lptr tb, auto N) { constexpr int n = N, s = n / 5, ct = n % 5; return ldstr<1280>(tb + ct * 64 + s * 5120); };
#define OWAIT(N, o) asm volatile("s_waitcnt vmcnt(%4) lgkmcnt(0)\n\ts_barrier" : "+v"(o[0]), "+v"(o[1]), "+v"(o[2]), "+v"(o[3]) : "n"(N) : "memory")
  static_for<0, 5>([&](auto P) { w_piece(P, 0, 0); });
  static_for<0, 4>([&](auto S) { o_load(S, 0, of[0][S]); });
  OWAIT(0, of[0]);
  { lptr tb = L + trb; static_for<0, 5>([&](auto I) { f[I] = wfrag(tb, I); }); }
  static_for<0, 24>([&](auto ST) {
    constexpr int st = ST, sl = st % 3, sn = (st + 1) % 3;
    lptr tb = L + (trb + sl * K2_STAGE), tn = L + (trb + sn * K2_STAGE);
    static_for<0, 20>([&](auto I) {
      constexpr int n = I, s = n / 5, ct = n % 5;
      acc[ct] = MFMA32(f[n % 5], of[sl][s], acc[ct]); PIN(acc[ct]);
      if constexpr (n + 5 < 20) f[n % 5] = wfrag(tb, std::integral_constant<int, (n + 5) % 20>{});
      if constexpr (st == 0 && n < 16) {
        if constexpr (n < 5) w_piece(IC(n), 1, 1); else if constexpr (n < 9) o_load(IC(n - 5), 1, of[1][n - 5]);
        else if constexpr (n < 12) w_piece(IC(n - 9), 2, 2);
        if constexpr (n == 12) { w_piece(IC(3), 2, 2); }
        if constexpr (n == 13) { w_piece(IC(4), 2, 2); o_load(IC(0), 2, of[2][0]); }
        if constexpr (n == 14) { o_load(IC(1), 2, of[2][1]); o_load(IC(2), 2, of[2][2]); }
        if constexpr (n == 15) o_load(IC(3), 2, of[2][3]);
      }
      if constexpr (st >= 1 && st + 2 < 24) {
        if constexpr (n == 1 || n == 4 || n == 7) w_piece(IC((n - 1) / 3 + 2), st + 2, (st + 2) % 3);
        if constexpr (n == 2 || n == 5 || n == 8 || n == 11) o_load(IC((n - 2) / 3), st + 2, of[(st + 2) % 3][(n - 2) / 3]);
      }
      if constexpr (K2TOUCH && n == 13 && st % 4 == 0 && st > 0 && st + K2D + 3 < 24) touch(st + K2D);
      if constexpr (n == 16 && st < 23) {
        constexpr int tc = (K2TOUCH && st % 4 == 0 && st > 0 && st + K2D + 3 < 24) ? 1 : 0;
        constexpr int tp = (K2TOUCH && st >= 2 && (st - 1) % 4 == 0 && st - 1 + K2D + 3 < 24) ? 1 : 0;
        constexpr int young = st == 0 ? 9 + tc : (st + 2 < 24 ? tp + 2 + 7 + tc : 0);
        OWAIT(young, of[sn]);
        f[0] = wfrag(tn, IC(0)); f[1] = wfrag(tn, IC(1));
      }
      if constexpr (n >= 17 && st < 23) f[n % 5] = wfrag(tn, std::integral_constant<int, n - 15>{});
      if constexpr ((n == 17 || n == 19) && st + 3 < 24) w_piece(IC((n - 17) / 2), st + 3, sl);
      SB();
    });
  });
  asm volatile("s_waitcnt vmcnt(0) lgkmcnt(0)\n\ts_barrier" : "+v"(tdummy) :: "memory");
  f32x4 keep4[10];
  {
    lptr out_l = L + (w * 10240 + lane * 16), in_l = L + ((w ^ 4) * 10240 + lane * 16);
    auto quad = [&](auto Q) { constexpr int q = Q; return f32x4{acc[(4 * q) >> 4][(4 * q) & 15], acc[(4 * q + 1) >> 4][(4 * q + 1) & 15], acc[(4 * q + 2) >> 4][(4 * q + 2) & 15], acc[(4 * q + 3) >> 4][(4 * q + 3) & 15]}; };
    if (ks2) static_for<0, 10>([&](auto I) { *(LAS f32x4*)(out_l + I * 1024) = quad(I); keep4[I] = quad(IC(10 + I)); });
    else     static_for<0, 10>([&](auto I) { *(LAS f32x4*)(out_l + I * 1024) = quad(IC(10 + I)); keep4[I] = quad(I); });
    asm volatile("s_waitcnt lgkmcnt(0)\n\ts_barrier" ::: "memory");
#pragma unroll
    for (int i = 0; i < 10; ++i) keep4[i] += *(const LAS f32x4*)(in_l + i * 1024);
  }
  const int pair = bb * 4 + nt;
  const bool keep = (rbk >> 1) == kh;
  const int slot_id = (pair * 4 + rbk) * 2 + ks2;
  const rsrc_t rs = mk_rsrc(part + (size_t)slot_id * 2560, 10240);
  if (!keep) {
#pragma unroll
    for (int i = 0; i < 10; ++i) __builtin_amdgcn_raw_buffer_store_b128(__builtin_bit_cast(u32x4, keep4[i]), rs, i * 1024 + lane * 16, 0, 16);
  } else {
    u32x4 in4[10];
    for (unsigned spins = 0; spins < (1u << 16); ++spins) {
      unsigned miss = 0;
      load10_sc1(in4, lane * 16, rs);
#pragma unroll
      for (int i = 0; i < 10; ++i) miss |= (in4[i][0] == EMPTY) | (in4[i][1] == EMPTY) | (in4[i][2] == EMPTY) | (in4[i][3] == EMPTY);
      if (__builtin_amdgcn_ballot_w64(miss != 0) == 0) break;
      __builtin_amdgcn_s_sleep(2);
      asm volatile("" ::: "memory");
    }
#pragma unroll
    for (int i = 0; i < 10; ++i) keep4[i] += __builtin_bit_cast(f32x4, in4[i]);
    char* orow = (char*)out + ((size_t)(b * SQ + 32 * rbk + (lane & 31)) * 1280 + br * 640 + nt * 160 + 8 * h) * 2 + ks2 * 160;
#pragma unroll
    for (int i = 0; i < 5; ++i) {
      const u32x4 pk = {cvtpk(keep4[2 * i][0], keep4[2 * i][1]), cvtpk(keep4[2 * i][2], keep4[2 * i][3]), cvtpk(keep4[2 * i + 1][0], keep4[2 * i + 1][1]), cvtpk(keep4[2 * i + 1][2], keep4[2 * i + 1][3])};
      *(u32x4*)(orow + i * 32) = pk;
    }
  }
}

constexpr size_t WS_OT = 0;
constexpr size_t OT_BYTES = (size_t)2 * NB * 4 * 384 * 1024;
constexpr size_t WS_PART = WS_OT + OT_BYTES;

extern "C" void kernel_launch(void* const* d_in, const int* in_sizes, int n_in, void* d_out, int out_size, void* d_ws, size_t ws_size, hipStream_t stream) {
  (void)in_sizes; (void)n_in; (void)out_size; (void)ws_size;
  const __bf16* xA = (const __bf16*)d_in[0]; const __bf16* kA = (const __bf16*)d_in[1]; const __bf16* vA = (const __bf16*)d_in[2];
  const __bf16* wqA = (const __bf16*)d_in[3]; const __bf16* woA = (const __bf16*)d_in[4];
  const __bf16* xB = (const __bf16*)d_in[5]; const __bf16* kB = (const __bf16*)d_in[6]; const __bf16* vB = (const __bf16*)d_in[7];
  const __bf16* wqB = (const __bf16*)d_in[8]; const __bf16* woB = (const __bf16*)d_in[9];
  char* ws = (char*)d_ws;
  (void)hipFuncSetAttribute((const void*)k1_qattn, hipFuncAttributeMaxDynamicSharedMemorySize, K1_LDS);
  (void)hipFuncSetAttribute((const void*)k2_oproj, hipFuncAttributeMaxDynamicSharedMemorySize, K2_LDS);
  hipLaunchKernelGGL(k1_qattn, dim3(256), dim3(512), K1_LDS, stream, xA, kA, vA, wqA, xB, kB, vB, wqB, (__bf16*)(ws + WS_OT), (unsigned*)(ws + WS_PART));
  hipLaunchKernelGGL(k2_oproj, dim3(256), dim3(512), K2_LDS, stream, (const __bf16*)(ws + WS_OT), woA, woB, (__bf16*)d_out, (float*)(ws + WS_PART));
}
```
